```python
import torch

def _softmax(x, axis=-1):
    return torch.softmax(x, dim=axis)
def _gelu(x):
    return torch.nn.functional.gelu(x, approximate="tanh")
def _silu(x):
    return torch.nn.functional.silu(x)
def _sigmoid(x):
    return torch.sigmoid(x)
def _layernorm(x, axis=-1, eps=1e-5):
    m = x.mean(dim=axis, keepdim=True)
    v = x.var(dim=axis, keepdim=True, unbiased=False)
    return (x - m) / torch.sqrt(v + eps)
def _scatter_add(tgt, idx, src, axis=0):
    return tgt.clone().index_add_(axis, idx, src)
def _segment_sum(src, seg, n):
    out = torch.zeros((n,) + src.shape[1:], dtype=src.dtype)
    return out.index_add_(0, seg, src)
def _pad(x, pw):
    flat = tuple(v for pair in reversed(pw) for v in pair)
    return torch.nn.functional.pad(x, flat)

kernel_name = 'standalone__gqa_softmax__near_ridge'

def setup_inputs():

    _w = (lambda a: (lambda t: t.to(torch.bfloat16) if t.is_floating_point() else t)(torch.as_tensor(a)))
    B, Hq, Hkv, Sq, Sk, D = 8, 32, 8, 128, 512, 128
    return {
        "q": _w((lambda *s: torch.randn(*s, dtype=torch.bfloat16) * 0.8)(B, Hq, Sq, D)),
        "k": _w((lambda *s: torch.randn(*s, dtype=torch.bfloat16) * 0.8)(B, Hkv, Sk, D)),
        "v": _w((lambda *s: torch.randn(*s, dtype=torch.bfloat16) * 0.05)(B, Hkv, Sk, D)),
    }

def reference(q, k, v):
    B, Hq, Sq, D = q.shape
    Hkv = k.shape[1]
    rep = Hq // Hkv
    k = (lambda x, n, axis: torch.repeat_interleave(x, n, dim=axis))(k, rep, axis=1)
    v = (lambda x, n, axis: torch.repeat_interleave(x, n, dim=axis))(v, rep, axis=1)
    scale = D ** -0.5
    scores = torch.einsum("bhqd,bhkd->bhqk", q, k) * scale
    p = _softmax(scores, axis=-1)
    return torch.einsum("bhqk,bhkd->bhqd", p, v)
```

```cpp
#include <hip/hip_runtime.h>
#include <hip/hip_bf16.h>

using bf16x8 = __attribute__((ext_vector_type(8))) short;
using s16x4  = __attribute__((ext_vector_type(4))) short;
using f32x16 = __attribute__((ext_vector_type(16))) float;
using f32x4  = __attribute__((ext_vector_type(4))) float;
using u32x4  = __attribute__((ext_vector_type(4))) unsigned;
using u32x2  = __attribute__((ext_vector_type(2))) unsigned;
typedef __attribute__((address_space(3))) const char* lds_cptr;
typedef short v4i16_t __attribute__((ext_vector_type(4)));
#define DI __device__ __forceinline__
#define MFMA(a, b, c) __builtin_amdgcn_mfma_f32_32x32x16_bf16(a, b, c, 0, 0, 0)

DI unsigned cvtpk(float lo, float hi) { unsigned r; asm("v_cvt_pk_bf16_f32 %0, %1, %2" : "=v"(r) : "v"(lo), "v"(hi)); return r; }
DI s16x4 vtr(lds_cptr p) { return __builtin_bit_cast(s16x4, __builtin_amdgcn_ds_read_tr16_b64_v4i16((__attribute__((address_space(3))) v4i16_t*)p)); }

constexpr float C2 = 0.08838834764831845f * 1.4426950408889634f;

__global__ __launch_bounds__(256, 1) void attn_simple(const unsigned short* __restrict__ Q, const unsigned short* __restrict__ K,
                                                      const unsigned short* __restrict__ V, unsigned short* __restrict__ O) {
  __shared__ __attribute__((aligned(16))) char lds[32768];
  const int tid = threadIdx.x, lane = tid & 63, wid = tid >> 6, r = lane & 31, h = lane >> 5;
  const int bid = blockIdx.x, xcd = bid & 7, slot = bid >> 3, grp = xcd * 8 + (slot >> 2), hq = grp * 4 + (slot & 3);
  const unsigned short* Qw = Q + ((size_t)hq * 128 + wid * 32 + r) * 128;
  const unsigned short* Kg = K + (size_t)grp * 512 * 128;
  const unsigned short* Vg = V + (size_t)grp * 512 * 128;
  bf16x8 qf[8];
#pragma unroll
  for (int s = 0; s < 8; ++s) qf[s] = *(const bf16x8*)(Qw + 16 * s + 8 * h);
  f32x16 o[4] = {};
  float m = -1e30f, l = 0.f;
  const lds_cptr L = (lds_cptr)lds;
  for (int t = 0; t < 8; ++t) {
    __syncthreads();
#pragma unroll
    for (int i = 0; i < 4; ++i) {
      const int n = tid + 256 * i, row = n >> 4, ch = n & 15;
      const u32x4 kv = *(const u32x4*)(Kg + (size_t)(t * 64 + row) * 128 + ch * 8);
      const u32x4 vv = *(const u32x4*)(Vg + (size_t)(t * 64 + row) * 128 + ch * 8);
      *(u32x4*)(lds + 2048 * (row >> 3) + 512 * (ch >> 2) + 64 * (row & 7) + 16 * ((ch & 3) ^ ((row >> 2) & 3))) = kv;
      *(u32x4*)(lds + 16384 + 2048 * (row >> 3) + 512 * (ch >> 2) + 64 * (row & 7) + 16 * (ch & 3)) = vv;
    }
    __syncthreads();
    f32x16 sacc[2] = {};
#pragma unroll
    for (int kb = 0; kb < 2; ++kb)
#pragma unroll
      for (int s = 0; s < 8; ++s) {
        const int ch = 2 * s + h;
        const bf16x8 kf = *(const bf16x8*)(lds + 8192 * kb + 2048 * (r >> 3) + 512 * (ch >> 2) + 64 * (r & 7) + 16 * ((ch & 3) ^ ((r >> 2) & 3)));
        sacc[kb] = MFMA(kf, qf[s], sacc[kb]);
      }
    float mx = sacc[0][0];
#pragma unroll
    for (int i = 1; i < 16; ++i) mx = fmaxf(mx, sacc[0][i]);
#pragma unroll
    for (int i = 0; i < 16; ++i) mx = fmaxf(mx, sacc[1][i]);
    { auto rr = __builtin_amdgcn_permlane32_swap(__float_as_uint(mx), __float_as_uint(mx), false, false);
      mx = fmaxf(__uint_as_float(rr[0]), __uint_as_float(rr[1])); }
    const float mn = fmaxf(m, mx * C2), al = __builtin_amdgcn_exp2f(m - mn);
    m = mn;
    float ps = 0.f;
#pragma unroll
    for (int kb = 0; kb < 2; ++kb)
#pragma unroll
      for (int i = 0; i < 16; ++i) { sacc[kb][i] = __builtin_amdgcn_exp2f(fmaf(sacc[kb][i], C2, -mn)); ps += sacc[kb][i]; }
    l = l * al + ps;
#pragma unroll
    for (int c = 0; c < 4; ++c)
#pragma unroll
      for (int i = 0; i < 16; ++i) o[c][i] *= al;
    const lds_cptr vb = L + 16384 + 64 * (4 * h + ((lane & 15) >> 2)) + 32 * ((lane >> 4) & 1) + 8 * (lane & 3);
#pragma unroll
    for (int ks = 0; ks < 4; ++ks) {
      const int kb = ks >> 1, b8 = 8 * (ks & 1);
      u32x4 pw = {cvtpk(sacc[kb][b8], sacc[kb][b8 + 1]), cvtpk(sacc[kb][b8 + 2], sacc[kb][b8 + 3]),
                  cvtpk(sacc[kb][b8 + 4], sacc[kb][b8 + 5]), cvtpk(sacc[kb][b8 + 6], sacc[kb][b8 + 7])};
      const bf16x8 pf = __builtin_bit_cast(bf16x8, pw);
#pragma unroll
      for (int c = 0; c < 4; ++c) {
        const s16x4 lo = vtr(vb + 4096 * ks + 512 * c), hi = vtr(vb + 4096 * ks + 2048 + 512 * c);
        const bf16x8 vf = {lo[0], lo[1], lo[2], lo[3], hi[0], hi[1], hi[2], hi[3]};
        o[c] = MFMA(vf, pf, o[c]);
      }
    }
  }
  { auto rr = __builtin_amdgcn_permlane32_swap(__float_as_uint(l), __float_as_uint(l), false, false);
    l = __uint_as_float(rr[0]) + __uint_as_float(rr[1]); }
  const float il = 1.f / l;
  unsigned short* Ow = O + ((size_t)hq * 128 + wid * 32 + r) * 128;
#pragma unroll
  for (int c = 0; c < 4; ++c)
#pragma unroll
    for (int g = 0; g < 4; ++g) {
      u32x2 w = {cvtpk(o[c][4 * g] * il, o[c][4 * g + 1] * il), cvtpk(o[c][4 * g + 2] * il, o[c][4 * g + 3] * il)};
      *(u32x2*)(Ow + 32 * c + 8 * g + 4 * h) = w;
    }
}

extern "C" void kernel_launch(void* const* d_in, const int* in_sizes, int n_in, void* d_out, int out_size, void* d_ws, size_t ws_size,
                              hipStream_t stream) {
  attn_simple<<<256, 256, 0, stream>>>((const unsigned short*)d_in[0], (const unsigned short*)d_in[1], (const unsigned short*)d_in[2],
                                       (unsigned short*)d_out);
}
```

```cpp
#include <hip/hip_runtime.h>
#include <hip/hip_bf16.h>
#include <utility>

using bf16x8 = __attribute__((ext_vector_type(8))) short;
using s16x4  = __attribute__((ext_vector_type(4))) short;
using f32x16 = __attribute__((ext_vector_type(16))) float;
using f32x4  = __attribute__((ext_vector_type(4))) float;
using f32x2  = __attribute__((ext_vector_type(2))) float;
using u32x4  = __attribute__((ext_vector_type(4))) unsigned;
using u32x2  = __attribute__((ext_vector_type(2))) unsigned;
typedef __attribute__((address_space(3))) char* lds_ptr;
typedef short v4i16_t __attribute__((ext_vector_type(4)));
template <int... Is> using iseq = std::integer_sequence<int, Is...>;
template <int N> using make_iseq = std::make_integer_sequence<int, N>;

#define DI __device__ __forceinline__
#define SBAR() __builtin_amdgcn_sched_barrier(0)
#define PIN(x) asm volatile("" : "+v"(x))
#define MFMA(a, b, c) __builtin_amdgcn_mfma_f32_32x32x16_bf16(a, b, c, 0, 0, 0)

constexpr float C2  = 0.08838834764831845f * 1.4426950408889634f;
constexpr float THR = 8.0f;
constexpr int SLOT = 32768, LDS_BYTES = 5 * SLOT;

typedef __bf16 bf16x2_t __attribute__((ext_vector_type(2)));
DI unsigned cvtpk(float lo, float hi) { f32x2 v = {lo, hi}; return __builtin_bit_cast(unsigned, __builtin_convertvector(v, bf16x2_t)); }
DI float fmax2(float a, float b) { return __builtin_elementwise_maximum(a, b); }
DI float fmax3(float a, float b, float c) { return fmax2(fmax2(a, b), c); }
DI float halfswap_max(float x) {
  auto rr = __builtin_amdgcn_permlane32_swap(__float_as_uint(x), __float_as_uint(x), false, false);
  return fmax2(__uint_as_float(rr[0]), __uint_as_float(rr[1]));
}
DI float halfswap_add(float x) {
  auto rr = __builtin_amdgcn_permlane32_swap(__float_as_uint(x), __float_as_uint(x), false, false);
  return __uint_as_float(rr[0]) + __uint_as_float(rr[1]);
}
DI bf16x8 ldsr128(lds_ptr p) { return *(const __attribute__((address_space(3))) bf16x8*)p; }
DI s16x4 ldstr(lds_ptr p) { return __builtin_bit_cast(s16x4, __builtin_amdgcn_ds_read_tr16_b64_v4i16((__attribute__((address_space(3))) v4i16_t*)p)); }

template <int LOFF, int GOFF> DI void dma(unsigned wl, unsigned wg, unsigned voff, __amdgpu_buffer_rsrc_t srd) {
  unsigned t;
  asm volatile("s_add_u32 m0, %1, %3\n\ts_add_u32 %0, %2, %4\n\tbuffer_load_dwordx4 %5, %6, %0 offen nt lds"
               : "=&s"(t) : "s"(wl), "s"(wg), "i"(LOFF), "i"(GOFF), "v"(voff), "s"(srd) : "memory", "scc");
}
template <int N> DI void wait_bar() { asm volatile("s_waitcnt vmcnt(%0)\n\ts_barrier" :: "i"(N) : "memory"); }

constexpr int BLK_IS_PV[16] = {0, 0, 0, 0, 1, 0, 1, 0, 1, 0, 1, 0, 1, 1, 1, 1};
constexpr int BLK_UNIT[16]  = {0, 1, 2, 3, 0, 4, 1, 5, 2, 6, 3, 7, 4, 5, 6, 7};
constexpr int KSTEP[4] = {1, 2, 4, 6}, VSTEP[4] = {3, 5, 7, 8};
constexpr int STEP_IS_V[9] = {0, 0, 0, 1, 0, 1, 0, 1, 1};
constexpr int STEP_TILE[9] = {0, 0, 1, 0, 2, 1, 3, 2, 3};
constexpr int STEP_ISSUE[9] = {-1, -1, -1, 0, 6, 20, 34, 46, 64};
constexpr int STEP_BAR[9] = {-1, -1, 11, 27, 35, 59, 67, 91, 107};
constexpr int RD_AHEAD = 4;

constexpr int pieces_issued_before(int slot) {
  int n = 12;
  for (int s = 3; s < 9; ++s) { int d = slot - STEP_ISSUE[s]; n += d <= 0 ? 0 : (d > 4 ? 4 : d); }
  return n;
}
constexpr int unit_lds(int blk) {
  const int u = BLK_UNIT[blk], st = BLK_IS_PV[blk] ? VSTEP[u >> 1] : KSTEP[u >> 1];
  return (st % 5) * SLOT + (u & 1) * 8192;
}

constexpr int NOPS = 9 + 4 + 7 * 7 + 3;
constexpr int op_pair(int k) { return k < 13 ? 0 : (k - 13) / 7 + 1; }
constexpr int op_kind(int k) { return k < 13 ? k - 9 : (k < 62 ? (k - 13) % 7 : k - 62 + 4); }
constexpr int op_cost(int k) { return k < 9 ? 1 : ((op_kind(k) == 2 || op_kind(k) == 3) ? 2 : 1); }
constexpr int SM_START = 9, PER_SLOT = 6;
constexpr int qk_done_slot(int u) { for (int b = 0; b < 16; ++b) if (!BLK_IS_PV[b] && BLK_UNIT[b] == u) return 8 * b + 8; return 0; }
constexpr int pv_slot(int u) { for (int b = 0; b < 16; ++b) if (BLK_IS_PV[b] && BLK_UNIT[b] == u) return 8 * b; return 0; }
constexpr int ops_through(int i) {
  int g = 0, budget = 0;
  for (int s = 0; s <= i; ++s) {
    if (s >= SM_START) budget += PER_SLOT;
    while (g < 8 * NOPS) {
      const int u = g / NOPS, k = g % NOPS;
      if (s < qk_done_slot(u) + 1) { budget = budget > PER_SLOT ? PER_SLOT : budget; break; }
      const int need = k < 13 + 4 * 7 ? pv_slot(u) : pv_slot(u) + 4;
      const bool forced = s + 1 >= need;
      if (!forced && budget < op_cost(k)) break;
      budget -= op_cost(k); ++g;
    }
  }
  return g;
}

#define STAMP(i) do { } while (0)
struct St {
  f32x16 o[4];
  f32x16 s[3];
  float x[16];
  unsigned p[3][8];
  bf16x8 q[8];
  bf16x8 fr[RD_AHEAD + 1];
  float mx, nm, thr, l0, l1;
  float alpha[8]; bool resc[8];
  lds_ptr kb[2][3], vb[3];
  unsigned wl, wgk, voffk, voffv;
  __amdgpu_buffer_rsrc_t srdK, srdV;
};

template <int I> DI void frag_read(St& c) {
  constexpr int blk = I >> 3, j = I & 7, base = unit_lds(blk), hi = base >> 16, lo = base & 65535;
  if constexpr (!BLK_IS_PV[blk]) {
    c.fr[I % (RD_AHEAD + 1)] = ldsr128(c.kb[j & 1][hi] + (lo + 512 * (j >> 1)));
  } else {
    constexpr int ks = j >> 2, cc = j & 3;
    const s16x4 a = ldstr(c.vb[hi] + (lo + 4096 * ks + 512 * cc)), b = ldstr(c.vb[hi] + (lo + 4096 * ks + 2048 + 512 * cc));
    c.fr[I % (RD_AHEAD + 1)] = (bf16x8){a[0], a[1], a[2], a[3], b[0], b[1], b[2], b[3]};
  }
}

template <int G> DI void sm_op(St& c) {
  constexpr int u = G / NOPS, k = G % NOPS, R = u % 3;
  f32x16& s = c.s[R];
  if constexpr (k == 0) c.mx = fmax3(s[0], s[1], s[2]);
  else if constexpr (k < 7) c.mx = fmax3(c.mx, s[2 * k + 1], s[2 * k + 2]);
  else if constexpr (k == 7) c.mx = fmax2(c.mx, s[15]);
  else if constexpr (k == 8) {
    if constexpr (u == 0) {
      const float m = halfswap_max(c.mx);
      c.nm = -m * C2; c.thr = m + THR / C2; c.resc[0] = false;
    } else {
      c.resc[u] = false;
      if (__builtin_expect(__any(!(c.mx <= c.thr)), 0)) {
        const float m = halfswap_max(c.mx);
        const float nn = -fmax2(m * C2, -c.nm);
        const float a = __builtin_amdgcn_exp2f(nn - c.nm);
        c.alpha[u] = a; c.l0 *= a; c.l1 *= a; c.nm = nn; c.thr = -nn * (1.0f / C2) + THR / C2; c.resc[u] = true;
      }
    }
  } else {
    constexpr int j = op_pair(k), t = op_kind(k), pj = j - 1;
    if constexpr (t < 2) c.x[2 * j + t] = __builtin_fmaf(s[2 * j + t], C2, c.nm);
    else if constexpr (t < 4) c.x[2 * j + t - 2] = __builtin_amdgcn_exp2f(c.x[2 * j + t - 2]);
    else if constexpr (t == 4) c.l0 += c.x[2 * pj];
    else if constexpr (t == 5) c.l1 += c.x[2 * pj + 1];
    else c.p[R][pj] = cvtpk(c.x[2 * pj], c.x[2 * pj + 1]);
  }
}
template <int G> DI void pin_op(St& c) {
  constexpr int u = G / NOPS, k = G % NOPS, R = u % 3;
  if constexpr (k < 8) PIN(c.mx);
  else if constexpr (k == 8) { PIN(c.nm); PIN(c.thr); }
  else {
    constexpr int j = op_pair(k), t = op_kind(k), pj = j - 1;
    if constexpr (t < 2) PIN(c.x[2 * j + t]);
    else if constexpr (t < 4) PIN(c.x[2 * j + t - 2]);
    else if constexpr (t == 4) PIN(c.l0);
    else if constexpr (t == 5) PIN(c.l1);
    else PIN(c.p[R][pj]);
  }
}
template <int LO, int... Ks> DI void pin_ops(St& c, iseq<Ks...>) { (pin_op<LO + Ks>(c), ...); }
template <int LO, int... Ks> DI void sm_ops(St& c, iseq<Ks...>) { (sm_op<LO + Ks>(c), ...); }

template <int STEPI, int PC> DI void dma_piece(St& c) {
  constexpr int half = PC >> 1, dh = PC & 1;
  constexpr int loff = (STEPI % 5) * SLOT + half * 16384 + dh * 1024;
  constexpr int goff = (half * 256 + STEP_TILE[STEPI] * 64) * 256 + dh * 128;
  if constexpr (STEP_IS_V[STEPI]) dma<loff, goff>(c.wl, c.wgk, c.voffv, c.srdV);
  else dma<loff, goff>(c.wl, c.wgk, c.voffk, c.srdK);
}

template <int I> DI void slot(St& c) {
  constexpr int blk = I >> 3, j = I & 7, u = BLK_UNIT[blk];
  if constexpr (j == 0) STAMP(3 + blk);
  {
    constexpr int st = STEP_BAR[2] == I ? 2 : STEP_BAR[3] == I ? 3 : STEP_BAR[4] == I ? 4 : STEP_BAR[5] == I ? 5 : STEP_BAR[6] == I ? 6 : STEP_BAR[7] == I ? 7 : STEP_BAR[8] == I ? 8 : -1;
    if constexpr (st >= 0) { wait_bar<pieces_issued_before(I) - 4 * (st + 1)>(); SBAR(); }
  }
  if constexpr (BLK_IS_PV[blk] && j == 0 && u > 0) {
    if (__builtin_expect(c.resc[u], 0)) {
#pragma unroll
      for (int cc = 0; cc < 4; ++cc)
#pragma unroll
        for (int r = 0; r < 16; ++r) c.o[cc][r] *= c.alpha[u];
    }
    SBAR();
  }
  if constexpr (!BLK_IS_PV[blk]) {
    if constexpr (j == 0) c.s[u % 3] = MFMA(c.fr[I % (RD_AHEAD + 1)], c.q[0], f32x16{});
    else c.s[u % 3] = MFMA(c.fr[I % (RD_AHEAD + 1)], c.q[j], c.s[u % 3]);
  } else {
    constexpr int ks = j >> 2, cc = j & 3;
    const u32x4 pw = {c.p[u % 3][4 * ks], c.p[u % 3][4 * ks + 1], c.p[u % 3][4 * ks + 2], c.p[u % 3][4 * ks + 3]};
    const bf16x8 pf = __builtin_bit_cast(bf16x8, pw);
    if constexpr (u == 0 && ks == 0) c.o[cc] = MFMA(c.fr[I % (RD_AHEAD + 1)], pf, f32x16{});
    else c.o[cc] = MFMA(c.fr[I % (RD_AHEAD + 1)], pf, c.o[cc]);
  }
  SBAR();
  if constexpr (I + RD_AHEAD < 128) frag_read<I + RD_AHEAD>(c);
  {
    constexpr int st = (I >= STEP_ISSUE[3] && I < STEP_ISSUE[3] + 4) ? 3 : (I >= STEP_ISSUE[4] && I < STEP_ISSUE[4] + 4) ? 4 : (I >= STEP_ISSUE[5] && I < STEP_ISSUE[5] + 4) ? 5
                     : (I >= STEP_ISSUE[6] && I < STEP_ISSUE[6] + 4) ? 6 : (I >= STEP_ISSUE[7] && I < STEP_ISSUE[7] + 4) ? 7 : (I >= STEP_ISSUE[8] && I < STEP_ISSUE[8] + 4) ? 8 : -1;
    if constexpr (st >= 0) dma_piece<st, I - STEP_ISSUE[st]>(c);
  }
  constexpr int lo = I == 0 ? 0 : ops_through(I - 1), hi = ops_through(I);
  if constexpr (hi > lo) {
    sm_ops<lo>(c, make_iseq<hi - lo>{});
    pin_ops<lo>(c, make_iseq<hi - lo>{});
  }
  SBAR();
}
template <int... Is> DI void slots(St& c, iseq<Is...>) { (slot<Is>(c), ...); }
template <int... Is> DI void first_reads(St& c, iseq<Is...>) { (frag_read<Is>(c), ...); }

__global__ __launch_bounds__(512, 1) void gqa_attn(const unsigned short* __restrict__ Q, const unsigned short* __restrict__ K,
                                                   const unsigned short* __restrict__ V, unsigned short* __restrict__ O
                                                   ) {
  extern __shared__ __attribute__((aligned(16))) char smem[];
  const lds_ptr L = (lds_ptr)smem;
  const int tid = threadIdx.x, lane = tid & 63, r = lane & 31, h = lane >> 5;
  const int wid = __builtin_amdgcn_readfirstlane(tid >> 6), qb = wid & 3, kh = wid >> 2;
  const int bid = blockIdx.x, xcd = bid & 7, sl = bid >> 3, grp = xcd * 8 + (sl >> 2), hq = grp * 4 + (sl & 3);
  St c;
  STAMP(0);
  const unsigned rowl = (lane >> 2) & 7, sub = lane >> 5;
  c.voffv = rowl * 256 + sub * 64 + (lane & 3) * 16;
  c.voffk = rowl * 256 + sub * 64 + (((lane & 3) ^ ((2 * (wid & 1) + (rowl >> 2)) & 3)) * 16);
  c.wl = (unsigned)(uintptr_t)L + wid * 2048;
  c.wgk = wid * 2048;
  const __amdgpu_buffer_rsrc_t srdQ = __builtin_amdgcn_make_buffer_rsrc((void*)(Q + (size_t)hq * 16384), (short)0, 32768, 0x00020000);
  c.srdK = __builtin_amdgcn_make_buffer_rsrc((void*)(K + (size_t)grp * 65536), (short)0, 131072, 0x00020000);
  c.srdV = __builtin_amdgcn_make_buffer_rsrc((void*)(V + (size_t)grp * 65536), (short)0, 131072, 0x00020000);
  dma<0, 0>(c.wl, c.wgk, c.voffk, srdQ); dma<1024, 128>(c.wl, c.wgk, c.voffk, srdQ);
  dma<16384, 16384>(c.wl, c.wgk, c.voffk, srdQ); dma<16384 + 1024, 16384 + 128>(c.wl, c.wgk, c.voffk, srdQ);
  dma_piece<1, 0>(c); dma_piece<1, 1>(c); dma_piece<1, 2>(c); dma_piece<1, 3>(c);
  dma_piece<2, 0>(c); dma_piece<2, 1>(c); dma_piece<2, 2>(c); dma_piece<2, 3>(c);
  {
    const unsigned rb = 2048 * (r >> 3) + 64 * (r & 7);
#pragma unroll
    for (int p = 0; p < 2; ++p) {
      const unsigned a = rb + 16 * ((2 * p + h) ^ ((r >> 2) & 3));
#pragma unroll
      for (int x = 0; x < 3; ++x) { c.kb[p][x] = L + (a + kh * 16384 + x * 65536); PIN(c.kb[p][x]); }
    }
    const unsigned va = 64 * (4 * h + ((lane & 15) >> 2)) + 32 * ((lane >> 4) & 1) + 8 * (lane & 3);
#pragma unroll
    for (int x = 0; x < 3; ++x) { c.vb[x] = L + (va + kh * 16384 + x * 65536); PIN(c.vb[x]); }
    c.l0 = 0.f; c.l1 = 0.f; c.mx = 0.f; c.nm = 0.f; c.thr = 0.f;
    STAMP(1);
    wait_bar<4>();
    SBAR();
    STAMP(2);
#pragma unroll
    for (int s = 0; s < 8; ++s) c.q[s] = ldsr128(L + (qb * 8192 + rb + 16 * ((2 * (s & 1) + h) ^ ((r >> 2) & 3)) + 512 * (s >> 1)));
  }
  first_reads(c, make_iseq<RD_AHEAD>{});
  SBAR();
  slots(c, make_iseq<128>{});
  STAMP(19);

  const float lsum = halfswap_add(c.l0 + c.l1);
  constexpr int XCH0 = 4 * SLOT, XCH1 = 0, STG = SLOT, ML = 2 * SLOT;
  {
    const lds_ptr xw = L + ((kh ? XCH1 : XCH0) + qb * 8192 + lane * 16);
    if (kh == 0) {
#pragma unroll
      for (int g = 0; g < 8; ++g) *(__attribute__((address_space(3))) f32x4*)(xw + g * 1024) = (f32x4){c.o[2 + (g >> 2)][4 * (g & 3)], c.o[2 + (g >> 2)][4 * (g & 3) + 1], c.o[2 + (g >> 2)][4 * (g & 3) + 2], c.o[2 + (g >> 2)][4 * (g & 3) + 3]};
    } else {
#pragma unroll
      for (int g = 0; g < 8; ++g) *(__attribute__((address_space(3))) f32x4*)(xw + g * 1024) = (f32x4){c.o[g >> 2][4 * (g & 3)], c.o[g >> 2][4 * (g & 3) + 1], c.o[g >> 2][4 * (g & 3) + 2], c.o[g >> 2][4 * (g & 3) + 3]};
      c.o[0] = c.o[2]; c.o[1] = c.o[3];
    }
    *(__attribute__((address_space(3))) f32x2*)(L + (ML + wid * 512 + lane * 8)) = (f32x2){c.nm, lsum};
  }
  STAMP(20);
  asm volatile("s_waitcnt lgkmcnt(0)\n\ts_barrier" ::: "memory");
  STAMP(21);
  {
    const f32x2 oml = *(const __attribute__((address_space(3))) f32x2*)(L + (ML + (wid ^ 4) * 512 + lane * 8));
    const lds_ptr xr = L + ((kh ? XCH0 : XCH1) + qb * 8192 + lane * 16);
    f32x4 x[8];
#pragma unroll
    for (int g = 0; g < 8; ++g) x[g] = *(const __attribute__((address_space(3))) f32x4*)(xr + g * 1024);
    const float nmin = fminf(c.nm, oml[0]);
    const float fa = __builtin_amdgcn_exp2f(nmin - c.nm), fb = __builtin_amdgcn_exp2f(nmin - oml[0]);
    const float il = __builtin_amdgcn_rcpf(lsum * fa + oml[1] * fb);
    const float ga = fa * il, gb = fb * il;
    const lds_ptr sw = L + (STG + wid * 4096 + r * 128 + 8 * h);
#pragma unroll
    for (int g = 0; g < 8; ++g) {
      float y[4];
#pragma unroll
      for (int e = 0; e < 4; ++e) y[e] = c.o[g >> 2][4 * (g & 3) + e] * ga + x[g][e] * gb;
      *(__attribute__((address_space(3))) u32x2*)(sw + 16 * (g ^ (r & 7))) = (u32x2){cvtpk(y[0], y[1]), cvtpk(y[2], y[3])};
    }
    asm volatile("s_waitcnt lgkmcnt(0)" ::: "memory");
    unsigned short* Ow = O + ((size_t)hq * 128 + qb * 32) * 128 + kh * 64;
#pragma unroll
    for (int i = 0; i < 4; ++i) {
      const int row = 8 * i + (lane >> 3), ch = lane & 7;
      const u32x4 w = *(const __attribute__((address_space(3))) u32x4*)(L + (STG + wid * 4096 + row * 128 + 16 * (ch ^ (row & 7))));
      __builtin_nontemporal_store(w, (u32x4*)(Ow + row * 128 + ch * 8));
    }
  }
}

extern "C" void kernel_launch(void* const* d_in, const int* in_sizes, int n_in, void* d_out, int out_size, void* d_ws, size_t ws_size,
                              hipStream_t stream) {
  (void)hipFuncSetAttribute((const void*)gqa_attn, hipFuncAttributeMaxDynamicSharedMemorySize, LDS_BYTES);
  gqa_attn<<<256, 512, LDS_BYTES, stream>>>((const unsigned short*)d_in[0], (const unsigned short*)d_in[1], (const unsigned short*)d_in[2],
                                            (unsigned short*)d_out
                                            );
}
```

```cpp
#include <hip/hip_runtime.h>
#include <hip/hip_bf16.h>
#include <utility>

using bf16x8 = __attribute__((ext_vector_type(8))) short;
using s16x4  = __attribute__((ext_vector_type(4))) short;
using f32x16 = __attribute__((ext_vector_type(16))) float;
using f32x4  = __attribute__((ext_vector_type(4))) float;
using f32x2  = __attribute__((ext_vector_type(2))) float;
using u32x4  = __attribute__((ext_vector_type(4))) unsigned;
using u32x2  = __attribute__((ext_vector_type(2))) unsigned;
typedef __attribute__((address_space(3))) char* lds_ptr;
typedef short v4i16_t __attribute__((ext_vector_type(4)));
typedef __bf16 bf16x2_t __attribute__((ext_vector_type(2)));
template <int... Is> using iseq = std::integer_sequence<int, Is...>;
template <int N> using make_iseq = std::make_integer_sequence<int, N>;

#define DI __device__ __forceinline__
#define LDS_AS __attribute__((address_space(3)))
#define SBAR() __builtin_amdgcn_sched_barrier(0)
#define PIN(x) asm volatile("" : "+v"(x))
#define MFMA(a, b, c) __builtin_amdgcn_mfma_f32_32x32x16_bf16(a, b, c, 0, 0, 0)
#define CBAR() asm volatile("" ::: "memory")
#define LDPOL " nt"
constexpr int STAUX = 16;

constexpr float C2 = 0.08838834764831845f * 1.4426950408889634f;
constexpr int TILE = 16384, LDS_BYTES = 10 * TILE;
constexpr int QSTG = 8 * TILE, OSTG = 6 * TILE;

DI unsigned cvtpk(float lo, float hi) { f32x2 v = {lo, hi}; return __builtin_bit_cast(unsigned, __builtin_convertvector(v, bf16x2_t)); }
DI float fmax2(float a, float b) { return __builtin_elementwise_maximum(a, b); }
DI float fmax3(float a, float b, float c) { return fmax2(fmax2(a, b), c); }
DI float halfswap_max(float x) {
  auto rr = __builtin_amdgcn_permlane32_swap(__float_as_uint(x), __float_as_uint(x), false, false);
  return fmax2(__uint_as_float(rr[0]), __uint_as_float(rr[1]));
}
DI float halfswap_add(float x) {
  auto rr = __builtin_amdgcn_permlane32_swap(__float_as_uint(x), __float_as_uint(x), false, false);
  return __uint_as_float(rr[0]) + __uint_as_float(rr[1]);
}
DI bf16x8 ldsr128(lds_ptr p) { return *(const LDS_AS bf16x8*)p; }
DI s16x4 ldstr(lds_ptr p) { return __builtin_bit_cast(s16x4, __builtin_amdgcn_ds_read_tr16_b64_v4i16((LDS_AS v4i16_t*)p)); }

template <int LOFF, int GOFF> DI void dma(unsigned wl, unsigned wg, unsigned voff, __amdgpu_buffer_rsrc_t srd) {
  unsigned t;
  asm volatile("s_add_u32 m0, %1, %3\n\ts_add_u32 %0, %2, %4\n\tbuffer_load_dwordx4 %5, %6, %0 offen" LDPOL " lds"
               : "=&s"(t) : "s"(wl), "s"(wg), "i"(LOFF), "i"(GOFF), "v"(voff), "s"(srd) : "memory", "scc");
}
template <int GOFF> DI void ldq(u32x4& d, unsigned wg, unsigned voff, __amdgpu_buffer_rsrc_t srd) {
  unsigned t;
  asm volatile("s_add_u32 %1, %2, %3\n\tbuffer_load_dwordx4 %0, %4, %5, %1 offen" LDPOL
               : "=&v"(d), "=&s"(t) : "s"(wg), "i"(GOFF), "v"(voff), "s"(srd) : "memory", "scc");
}
template <int N> DI void wait_reg(u32x4& d) { asm volatile("s_waitcnt vmcnt(%1)" : "+v"(d) : "i"(N) : "memory"); }
DI u32x4 scale_q(u32x4 w) {
  u32x4 r;
#pragma unroll
  for (int e = 0; e < 4; ++e) r[e] = cvtpk(__uint_as_float(w[e] << 16) * C2, __uint_as_float(w[e] & 0xffff0000u) * C2);
  return r;
}
DI void bar() { asm volatile("s_barrier" ::: "memory"); }

constexpr int NS = 256;
constexpr int RD_AHEAD = 6;
constexpr int NOPS = 762;
constexpr unsigned short OPS[NOPS] = {
  1792,1793,1794,1795,1796,1797,1798,1799,2048,2816,2817,2818,2819,2820,2821,2822,2823,2824,2825,2826,2827,2828,2829,2830,2831,0,1,512,2,513,3,1536,514,
  1024,4,515,5,516,6,1537,517,1025,7,1026,518,1027,8,1028,519,1538,9,1029,520,1030,10,521,11,1539,522,1540,12,1031,523,1032,13,1033,524,1541,14,1034,
  525,1035,15,526,527,1542,16,1036,17,1037,528,18,529,19,1543,530,1038,20,1039,531,1552,21,532,1553,22,1040,533,1041,23,1042,534,24,1554,535,1043,25,
  1044,536,1045,26,537,1555,27,1046,538,1047,28,1048,539,29,1556,540,1557,30,1049,541,31,542,1558,543,1050,1051,1052,1053,544,1559,545,1054,546,1055,
  547,548,1568,549,1569,550,1056,1057,1058,551,1570,552,1059,553,1060,1061,554,1571,555,1572,556,1062,1063,1064,557,1573,558,1065,559,1066,1067,1574,
  1575,1068,1069,1070,1071,560,561,562,563,564,565,1584,566,1585,567,1072,1073,1074,1075,568,1586,569,1587,570,1076,1077,1078,571,1588,572,1079,573,
  1080,574,1081,575,1589,1590,1082,1083,1084,1085,1086,1591,1087,576,577,578,579,580,581,1600,582,1601,583,1088,1089,1090,1091,584,1602,585,1603,586,
  1092,1093,1094,587,1604,588,1095,589,1096,590,1097,591,1605,1606,1098,1099,1100,1101,1102,1607,1103,592,593,1104,594,595,1616,1105,596,1617,597,598,
  1618,1106,599,1107,1108,1109,600,1619,1110,601,1111,602,603,1620,1112,604,1621,1113,605,1114,606,1622,1115,607,1116,1623,1117,1118,1119,608,609,1120,
  610,611,1632,1121,612,1633,613,614,1634,1122,615,1123,1124,1125,616,1635,617,1126,618,619,1636,1127,620,1637,1128,621,1129,622,1638,1130,623,1131,
  1639,1132,1133,1134,1135,624,625,1136,626,627,1648,1137,628,1649,629,630,1650,1138,631,1139,1140,1141,632,1651,1142,633,1143,634,635,1652,1144,636,
  1653,1145,637,1146,638,1654,1147,639,1148,1655,1149,1150,1151,640,641,1152,642,643,1664,1153,644,1665,645,646,1666,1154,647,1155,1156,1157,648,1667,
  649,1158,650,651,1668,1159,652,1669,1160,653,1161,654,1670,1162,655,1163,1671,1164,1165,1166,1167,656,657,1168,658,659,1680,1169,660,1681,661,662,
  1682,1170,663,1171,1172,1173,664,1683,1174,665,1175,666,667,1684,1176,668,1685,1177,669,1178,670,1686,1179,671,1180,1687,1181,1182,1183,672,673,1184,
  674,675,1696,1185,676,1697,677,678,1698,1186,679,1187,1188,1189,680,1699,681,1190,682,683,1700,1191,684,1701,1192,685,1193,686,1702,1194,687,1195,
  1703,1196,1197,1198,1199,688,689,1200,690,691,1712,1201,692,1713,693,694,1714,1202,695,1203,1204,1205,696,1715,1206,697,1207,698,699,1716,1208,700,
  1717,1209,701,1210,702,1718,1211,703,1212,1719,1213,1214,1215,704,705,1216,706,707,1728,1217,708,1729,709,710,1730,1218,711,1219,1220,1221,712,1731,
  713,1222,714,715,1732,1223,716,1733,1224,717,1225,718,1734,1226,719,1227,1735,1228,1229,1230,1231,720,721,1232,722,723,1744,1233,724,1745,725,726,
  1746,1234,727,1235,1236,1237,728,1747,1238,729,1239,730,731,1748,1240,732,1749,1241,733,1242,734,1750,1243,735,1244,1751,1245,1246,1247,736,737,1248,
  738,739,1760,1249,740,1761,741,742,1762,1250,743,1251,1252,1253,744,1763,745,1254,746,747,1764,1255,748,1765,1256,749,1257,750,1766,1258,751,1259,
  1767,1260,1261,1262,1263,752,753,754,755,1776,756,1777,1264,757,1265,758,1778,1266,759,1267,760,1779,1268,761,1269,762,1780,1270,763,1271,764,1781,
  1272,765,1273,1782,766,767,1783,1274,1275,1276,1277,1278,1279,3328,3584,3585,3586,3840,3587,3588,3589,3841,3842,3590,3591,3843,3600,3601,3602,3856,
  3603,3604,3605,3857,3858,3606,3607,3859,4096,4097,4098,4099,4352,3616,3617,4353,3872,3618,3619,3620,4354,3873,3621,3622,3623,4355,3874,3875,3632,3633,
  3888,3634,3635,3889,3636,3637,3890,3638,3639,3891,4112,4113,4114,4115,4368,4369,4370,4371,};
constexpr unsigned short THRU[256] = {
  0,0,0,0,0,0,0,0,0,8,9,20,30,38,48,56,66,74,83,92,101,110,119,127,134,141,149,156,164,171,177,177,177,182,191,199,207,215,217,217,217,217,217,217,217,
  217,217,217,217,222,231,239,247,255,257,257,257,257,257,257,257,257,257,257,257,258,261,264,267,270,274,277,280,283,286,288,291,293,297,297,297,298,
  301,304,307,310,314,316,319,322,325,327,330,332,336,337,337,338,341,344,347,350,354,357,360,363,366,368,371,373,377,377,377,378,381,384,387,390,394,
  396,399,402,405,407,410,412,416,417,417,418,421,424,427,430,434,437,440,443,446,448,451,453,457,457,457,458,461,464,467,470,474,476,479,482,485,487,
  490,492,496,497,497,498,501,504,507,510,514,517,520,523,526,528,531,533,537,537,537,538,541,544,547,550,554,556,559,562,565,567,570,572,576,577,577,
  578,581,584,587,590,594,597,600,603,606,608,611,613,617,617,617,618,621,624,627,630,634,636,639,642,645,647,650,652,656,657,657,658,660,662,665,667,
  670,672,675,677,680,682,685,691,695,697,698,701,705,709,713,717,721,724,726,729,734,739,742,742,742,742,};
constexpr unsigned char PADS[256] = {
  0,0,0,0,0,0,0,0,12,3,7,0,0,0,0,0,0,0,0,0,0,0,0,0,3,0,0,0,0,0,5,10,11,0,0,0,0,2,10,11,12,11,11,10,11,10,11,10,11,0,0,0,0,2,10,10,0,0,0,0,0,0,0,0,0,0,0,
  0,0,0,0,0,0,0,0,0,0,0,0,0,0,0,0,0,0,0,0,0,0,0,0,0,0,0,0,0,0,0,0,0,0,0,0,0,0,0,0,0,0,0,0,0,0,0,0,0,0,0,0,0,0,0,0,0,0,0,0,0,0,0,0,0,0,0,0,0,0,0,0,0,0,0,
  0,0,0,0,0,0,0,0,0,0,0,0,0,0,0,0,0,0,0,0,0,0,0,0,0,0,0,0,0,0,0,0,0,0,0,0,0,0,0,0,0,0,0,0,0,0,0,0,0,0,0,0,0,0,0,0,0,0,0,0,0,0,0,0,0,0,0,0,0,0,0,0,0,0,0,
  0,0,0,0,0,0,0,0,0,0,0,0,0,0,0,0,0,0,0,0,0,0,0,0,0,0,0,0,0,0,0,0,0,0,0,0,0,0,0,};
constexpr int NBAR = 9;
constexpr int BAR_SLOT[NBAR] = {-1, 8, 24, 56, 88, 120, 152, 184, 216};
constexpr int BAR_PUB[NBAR] = {0, 1, 3, 5, 7, 9, 11, 13, 15};
constexpr int BAR_ISS[NBAR] = {2, 3, 4, 6, 8, 10, 13, 15, 15};
constexpr unsigned char SEQ[16] = {0, 1, 8, 2, 9, 3, 10, 4, 11, 5, 12, 6, 13, 7, 14, 15};
constexpr int NPRE = 6;
constexpr short PRE_RD[NPRE] = {1, 0, 3, 2, 5, 4};
constexpr short RDA[256] = {
  7,6,9,8,11,10,13,12,15,14,17,16,19,18,21,20,23,22,25,24,27,26,29,28,31,30,33,32,35,34,37,36,39,38,41,40,43,42,45,44,47,46,49,48,51,50,53,52,55,54,57,
  56,59,58,61,60,63,62,65,64,67,66,69,68,71,70,73,72,75,74,77,76,79,78,81,80,83,82,85,84,87,86,89,88,91,90,93,92,95,94,97,96,99,98,101,100,103,102,105,
  104,107,106,109,108,111,110,113,112,115,114,117,116,119,118,121,120,123,122,125,124,127,126,129,128,131,130,133,132,135,134,137,136,139,138,141,140,
  143,142,145,144,147,146,149,148,151,150,153,152,155,154,157,156,159,158,161,160,163,162,165,164,167,166,169,168,171,170,173,172,175,174,177,176,179,
  178,181,180,183,182,185,184,187,186,189,188,191,190,193,192,195,194,197,196,199,198,201,200,203,202,205,204,207,206,209,208,211,210,213,212,215,214,
  217,216,219,218,221,220,223,222,225,224,227,226,229,228,231,230,233,232,235,234,237,236,239,238,241,240,243,242,245,244,247,246,249,248,251,250,253,
  252,255,254,-1,-1,-1,-1,-1,-1,};
constexpr short RDB[256] = {
  -1,-1,-1,-1,-1,-1,-1,-1,-1,-1,-1,-1,-1,-1,-1,-1,-1,-1,-1,-1,-1,-1,-1,-1,-1,-1,-1,-1,-1,-1,-1,-1,-1,-1,-1,-1,-1,-1,-1,-1,-1,-1,-1,-1,-1,-1,-1,-1,-1,-1,
  -1,-1,-1,-1,-1,-1,-1,-1,-1,-1,-1,-1,-1,-1,-1,-1,-1,-1,-1,-1,-1,-1,-1,-1,-1,-1,-1,-1,-1,-1,-1,-1,-1,-1,-1,-1,-1,-1,-1,-1,-1,-1,-1,-1,-1,-1,-1,-1,-1,-1,
  -1,-1,-1,-1,-1,-1,-1,-1,-1,-1,-1,-1,-1,-1,-1,-1,-1,-1,-1,-1,-1,-1,-1,-1,-1,-1,-1,-1,-1,-1,-1,-1,-1,-1,-1,-1,-1,-1,-1,-1,-1,-1,-1,-1,-1,-1,-1,-1,-1,-1,
  -1,-1,-1,-1,-1,-1,-1,-1,-1,-1,-1,-1,-1,-1,-1,-1,-1,-1,-1,-1,-1,-1,-1,-1,-1,-1,-1,-1,-1,-1,-1,-1,-1,-1,-1,-1,-1,-1,-1,-1,-1,-1,-1,-1,-1,-1,-1,-1,-1,-1,
  -1,-1,-1,-1,-1,-1,-1,-1,-1,-1,-1,-1,-1,-1,-1,-1,-1,-1,-1,-1,-1,-1,-1,-1,-1,-1,-1,-1,-1,-1,-1,-1,-1,-1,-1,-1,-1,-1,-1,-1,-1,-1,-1,-1,-1,-1,-1,-1,-1,-1,
  -1,-1,-1,-1,-1,-1,};
constexpr unsigned short SLOT[256] = {
  0,16,32,48,64,80,96,112,256,272,288,304,320,336,352,368,512,528,544,560,576,592,608,624,768,784,800,816,832,848,864,880,4096,4097,4098,4099,4112,4113,
  4114,4115,1024,1040,1056,1072,1088,1104,1120,1136,4352,4353,4354,4355,4368,4369,4370,4371,1280,1296,1312,1328,1344,1360,1376,1392,4608,4609,4610,4611,
  4624,4625,4626,4627,1536,1552,1568,1584,1600,1616,1632,1648,4864,4865,4866,4867,4880,4881,4882,4883,1792,1808,1824,1840,1856,1872,1888,1904,5120,5121,
  5122,5123,5136,5137,5138,5139,2048,2064,2080,2096,2112,2128,2144,2160,5376,5377,5378,5379,5392,5393,5394,5395,2304,2320,2336,2352,2368,2384,2400,2416,
  5632,5633,5634,5635,5648,5649,5650,5651,2560,2576,2592,2608,2624,2640,2656,2672,5888,5889,5890,5891,5904,5905,5906,5907,2816,2832,2848,2864,2880,2896,
  2912,2928,6144,6145,6146,6147,6160,6161,6162,6163,3072,3088,3104,3120,3136,3152,3168,3184,6400,6401,6402,6403,6416,6417,6418,6419,3328,3344,3360,3376,
  3392,3408,3424,3440,6656,6657,6658,6659,6672,6673,6674,6675,3584,3600,3616,3632,3648,3664,3680,3696,6912,6913,6914,6915,6928,6929,6930,6931,3840,3856,
  3872,3888,3904,3920,3936,3952,7168,7169,7184,7185,7424,7425,7440,7441,7680,7681,7696,7697,7936,7937,7952,7953,7170,7186,7426,7442,7682,7698,7938,7954,
  7171,7187,7427,7443,7683,7699,7939,7955,};
constexpr bool s_pv(int i) { return (SLOT[i] >> 12) != 0; }
constexpr int s_unit(int i) { return (SLOT[i] >> 8) & 15; }
constexpr int s_ks(int i) { return (SLOT[i] >> 4) & 15; }
constexpr int s_cc(int i) { return SLOT[i] & 15; }
constexpr bool seq_is_v(int i) { return SEQ[i] >= 8; }
constexpr int seq_tile(int i) { return SEQ[i] & 7; }
constexpr int seq_of(int code) { for (int i = 0; i < 16; ++i) if (SEQ[i] == code) return i; return 0; }
constexpr int seq_slot(int i) { return i < 10 ? i : i - 10; }
constexpr int PRE = BAR_ISS[0];
constexpr bool bar_at(int slot) { for (int b = 1; b < NBAR; ++b) if (BAR_SLOT[b] == slot) return true; return false; }
constexpr int NFR = RD_AHEAD + 2;
constexpr int unit_lds(int i) {
  const int u = s_unit(i), sq = seq_of((s_pv(i) ? 8 : 0) + (u >> 1));
  return seq_slot(sq) * TILE + (u & 1) * 8192;
}

constexpr float BIG = 65536.0f * 64.0f;
constexpr float LIM = 16.0f;

struct St {
  f32x16 o[4];
  f32x16 s[16];
  float x[16][16];
  unsigned ob[4][8];
  unsigned rx;
  lds_ptr sa[8];
  u32x4 w[2][4];
  unsigned p[16][8];
  bf16x8 q[8];
  bf16x8 fr[NFR];
  f32x16 negm;
  float mx, nm, l0, l1, lsum, il;
  lds_ptr kb[2][3], vb[3], sw, sr;
  __amdgpu_buffer_rsrc_t srdO; unsigned ovo;
  const unsigned short *gq, *gk, *gv;
};

template <int I> DI void frag_read(St& c) {
  constexpr int base = unit_lds(I), hi = base >> 16, lo = base & 65535;
  if constexpr (!s_pv(I)) {
    constexpr int j = s_ks(I);
    c.fr[I % NFR] = ldsr128(c.kb[j & 1][hi] + (lo + 512 * (j >> 1)));
  } else {
    constexpr int ks = s_ks(I), cc = s_cc(I);
    const s16x4 a = ldstr(c.vb[hi] + (lo + 4096 * ks + 512 * cc)), b = ldstr(c.vb[hi] + (lo + 4096 * ks + 2048 + 512 * cc));
    c.fr[I % NFR] = (bf16x8){a[0], a[1], a[2], a[3], b[0], b[1], b[2], b[3]};
  }
}

template <int G> DI void sm_op(St& c) {
  constexpr int kind = OPS[G] >> 8, u = (OPS[G] >> 4) & 15, k = OPS[G] & 15;
  f32x16& s = c.s[u];
  if constexpr (kind == 7) {
    if constexpr (k == 0) c.mx = fmax3(s[0], s[1], s[2]);
    else if constexpr (k < 7) c.mx = fmax3(c.mx, s[2 * k + 1], s[2 * k + 2]);
    else c.mx = fmax2(c.mx, s[15]);
  } else if constexpr (kind == 8) {
    c.nm = -halfswap_max(c.mx);
  } else if constexpr (kind == 11) {
    float t = c.nm; PIN(t); c.negm[k] = t;
  } else if constexpr (kind == 0) {
    c.x[u][k] = s[k] + c.nm;
  } else if constexpr (kind == 2) {
    if constexpr (u < 2) c.x[u][k] = __builtin_amdgcn_exp2f(c.x[u][k]);
    else c.x[u][k] = __builtin_amdgcn_exp2f(s[k]);
  } else if constexpr (kind == 4) {
    if constexpr (u == 0 && k == 0) c.l0 = c.x[0][0];
    else if constexpr (u == 0 && k == 1) c.l1 = c.x[0][1];
    else if constexpr ((k & 1) == 0) c.l0 += c.x[u][k];
    else c.l1 += c.x[u][k];
  } else if constexpr (kind == 6) {
    c.p[u][k] = cvtpk(c.x[u][2 * k], c.x[u][2 * k + 1]);
  } else if constexpr (kind == 13) {
    c.lsum = halfswap_add(c.l0 + c.l1);
    c.il = __builtin_amdgcn_rcpf(c.lsum);
  } else if constexpr (kind == 14) {
    float a = c.o[u][2 * k] * c.il, b = c.o[u][2 * k + 1] * c.il;
    if constexpr (G < THRU[NS - 1]) { PIN(a); PIN(b); }
    c.ob[u][k] = cvtpk(a, b);
  } else if constexpr (kind == 15) {
    *(LDS_AS u32x2*)(c.sa[4 * (u & 1) + k] + 128 * (u >> 1)) = (u32x2){c.ob[u][2 * k], c.ob[u][2 * k + 1]};
    if constexpr (G < THRU[NS - 1]) SBAR();
  } else if constexpr (kind == 16) {
    CBAR();
    c.w[u][k] = *(const LDS_AS u32x4*)(c.sr + (k * 2048 + u * 128));
    SBAR();
  } else if constexpr (kind == 17) {
    __builtin_amdgcn_raw_buffer_store_b128(c.w[u][k], c.srdO, c.ovo, k * 2048 + u * 128, STAUX);
    SBAR();
  }
}
constexpr bool later_same(int g, int hi, int kind, int par) {
  for (int t = g + 1; t < hi; ++t) if ((OPS[t] >> 8) == kind && (par < 0 || (OPS[t] & 1) == par)) return true;
  return false;
}
constexpr bool later_op(int g, int hi, int code) { for (int t = g + 1; t < hi; ++t) if (OPS[t] == code) return true; return false; }
template <int G, int HI> DI void pin_op(St& c) {
  constexpr int kind = OPS[G] >> 8, u = (OPS[G] >> 4) & 15, k = OPS[G] & 15;
  if constexpr (kind == 7) { if constexpr (!later_same(G, HI, 7, -1)) PIN(c.mx); }
  else if constexpr (kind == 8) PIN(c.nm);
  else if constexpr (kind == 11) { if constexpr (k == 15) PIN(c.negm); }
  else if constexpr (kind == 0) { if constexpr (!later_op(G, HI, 2 * 256 + u * 16 + k)) PIN(c.x[u][k]); }
  else if constexpr (kind == 2) PIN(c.x[u][k]);
  else if constexpr (kind == 4) {
    if constexpr (!(u == 0 && k < 2) && !later_same(G, HI, 4, k & 1)) { if constexpr ((k & 1) == 0) PIN(c.l0); else PIN(c.l1); }
  }
  else if constexpr (kind == 6) PIN(c.p[u][k]);
  else if constexpr (kind == 13) PIN(c.il);
  else if constexpr (kind == 14) { if constexpr (!later_op(G, HI, 15 * 256 + u * 16 + (k >> 1))) PIN(c.ob[u][k]); }
}
template <int LO, int HI, int... Ks> DI void pin_ops(St& c, iseq<Ks...>) { (pin_op<LO + Ks, HI>(c), ...); }
template <int LO, int... Ks> DI void sm_ops(St& c, iseq<Ks...>) { (sm_op<LO + Ks>(c), ...); }

template <int CC> DI void stage_block(St& c, unsigned rx) {
#pragma unroll
  for (int g = 0; g < 4; ++g) {
    const u32x2 w = {cvtpk(c.o[CC][4 * g] * c.il, c.o[CC][4 * g + 1] * c.il), cvtpk(c.o[CC][4 * g + 2] * c.il, c.o[CC][4 * g + 3] * c.il)};
    *(LDS_AS u32x2*)(c.sw + ((16 * (4 * (CC & 1) + g)) ^ rx) + 128 * (CC >> 1)) = w;
  }
}
template <int HF> DI void read_half(St& c, u32x4* w) {
#pragma unroll
  for (int i = 0; i < 4; ++i) w[i] = *(const LDS_AS u32x4*)(c.sr + (i * 2048 + HF * 128));
}
template <int HF> DI void store_half(St& c, const u32x4* w) {
#pragma unroll
  for (int i = 0; i < 4; ++i) __builtin_amdgcn_raw_buffer_store_b128(w[i], c.srdO, c.ovo, i * 2048 + HF * 128, STAUX);
}

DI void exact_path(St& c, lds_ptr xb) {
  const int lane = threadIdx.x & 63, r = lane & 31, h = lane >> 5;
  f32x16 o[4] = {};
  float m = -__builtin_inff(), l = 0.f;
  const lds_ptr vr = xb + (64 * (4 * h + ((lane & 15) >> 2)) + 32 * ((lane >> 4) & 1) + 8 * (lane & 3));
#pragma unroll 1
  for (int u = 0; u < 16; ++u) {
    f32x16 s = {};
    unsigned qo = r * 128 + 8 * h; PIN(qo);
#pragma unroll
    for (int j = 0; j < 8; ++j) {
      s = MFMA(*(const bf16x8*)(c.gk + (u * 32 + r) * 128 + 16 * j + 8 * h), *(const bf16x8*)(c.gq + qo + 16 * j), s);
      if (j == 3) SBAR();
    }
    SBAR();
#pragma unroll
    for (int i = 0; i < 8; ++i) {
      const int n = i * 64 + lane, row = n >> 4, ch = n & 15;
      *(LDS_AS u32x4*)(xb + (2048 * (row >> 3) + 512 * (ch >> 2) + 64 * (row & 7) + 16 * (ch & 3))) = *(const u32x4*)(c.gv + (u * 32 + row) * 128 + ch * 8);
    }
    float mx = s[0];
#pragma unroll
    for (int i = 1; i < 16; ++i) mx = fmax2(mx, s[i]);
    const float mn = fmax2(m, halfswap_max(mx) * C2), a = __builtin_amdgcn_exp2f(m - mn);
    m = mn;
    float ps = 0.f;
#pragma unroll
    for (int i = 0; i < 16; ++i) { s[i] = __builtin_amdgcn_exp2f(__builtin_fmaf(s[i], C2, -mn)); ps += s[i]; }
    l = l * a + ps;
#pragma unroll
    for (int cc = 0; cc < 4; ++cc)
#pragma unroll
      for (int i = 0; i < 16; ++i) o[cc][i] *= a;
    asm volatile("s_waitcnt lgkmcnt(0)" ::: "memory");
#pragma unroll
    for (int ks = 0; ks < 2; ++ks) {
      const u32x4 pw = {cvtpk(s[8 * ks], s[8 * ks + 1]), cvtpk(s[8 * ks + 2], s[8 * ks + 3]), cvtpk(s[8 * ks + 4], s[8 * ks + 5]), cvtpk(s[8 * ks + 6], s[8 * ks + 7])};
#pragma unroll
      for (int cc = 0; cc < 4; ++cc) {
        const s16x4 a0 = ldstr(vr + (4096 * ks + 512 * cc)), b0 = ldstr(vr + (4096 * ks + 2048 + 512 * cc));
        o[cc] = MFMA(((bf16x8){a0[0], a0[1], a0[2], a0[3], b0[0], b0[1], b0[2], b0[3]}), __builtin_bit_cast(bf16x8, pw), o[cc]);
      }
    }
    asm volatile("s_waitcnt lgkmcnt(0)" ::: "memory");
  }
  c.il = __builtin_amdgcn_rcpf(halfswap_add(l));
#pragma unroll
  for (int cc = 0; cc < 4; ++cc) c.o[cc] = o[cc];
  const unsigned rx = 16 * (r & 7);
  stage_block<0>(c, rx); stage_block<1>(c, rx); stage_block<2>(c, rx); stage_block<3>(c, rx);
  CBAR();
  u32x4 w[4];
  read_half<0>(c, w); store_half<0>(c, w); read_half<1>(c, w); store_half<1>(c, w);
}

template <int I> DI void slot(St& c) {
  constexpr int u = s_unit(I);
  if constexpr (bar_at(I)) {
    bar();
    SBAR();
  }
  if constexpr (!s_pv(I)) {
    constexpr int j = s_ks(I);
    if constexpr (j == 0 && u < 2) c.s[u] = MFMA(c.fr[I % NFR], c.q[0], f32x16{});
    else if constexpr (j == 0) c.s[u] = MFMA(c.fr[I % NFR], c.q[0], c.negm);
    else c.s[u] = MFMA(c.fr[I % NFR], c.q[j], c.s[u]);
  } else {
    constexpr int ks = s_ks(I), cc = s_cc(I);
    const u32x4 pw = {c.p[u][4 * ks], c.p[u][4 * ks + 1], c.p[u][4 * ks + 2], c.p[u][4 * ks + 3]};
    const bf16x8 pf = __builtin_bit_cast(bf16x8, pw);
    if constexpr (u == 0 && ks == 0) c.o[cc] = MFMA(c.fr[I % NFR], pf, f32x16{});
    else c.o[cc] = MFMA(c.fr[I % NFR], pf, c.o[cc]);
  }
  SBAR();
  if constexpr (RDA[I] >= 0) frag_read<RDA[I]>(c);
  if constexpr (RDB[I] >= 0) frag_read<RDB[I]>(c);
  SBAR();
  if constexpr (I < 8) { c.sa[I] = c.sw + ((16 * I) ^ c.rx); PIN(c.sa[I]); }
  constexpr int lo = I == 0 ? 0 : THRU[I - 1], hi = THRU[I];
  if constexpr (hi > lo) {
    sm_ops<lo>(c, make_iseq<hi - lo>{});
    pin_ops<lo, hi>(c, make_iseq<hi - lo>{});
  }
  if constexpr (PADS[I] > 0) { SBAR(); asm volatile("s_nop %0" :: "i"(PADS[I] - 1)); }
  SBAR();
}
template <int B, int... Is> DI void slots(St& c, iseq<Is...>) { (slot<B + Is>(c), ...); }
template <int... Is> DI void first_reads(St& c, iseq<Is...>) { (frag_read<PRE_RD[Is]>(c), ...); }

struct Ld { unsigned wl, wg, voffk, voffv; __amdgpu_buffer_rsrc_t srdK, srdV; };
template <int SQ> DI void gap() { if constexpr (SQ > 3) __builtin_amdgcn_s_sleep(1); }
template <int SQ> DI void load_tile(const Ld& d) {
  constexpr int lo = seq_slot(SQ) * TILE, go = seq_tile(SQ) * TILE;
  if constexpr (seq_is_v(SQ)) {
    dma<lo, go>(d.wl, d.wg, d.voffv, d.srdV); gap<SQ>(); dma<lo + 1024, go + 128>(d.wl, d.wg, d.voffv, d.srdV); gap<SQ>();
    dma<lo + 8192, go + 8192>(d.wl, d.wg, d.voffv, d.srdV); gap<SQ>(); dma<lo + 8192 + 1024, go + 8192 + 128>(d.wl, d.wg, d.voffv, d.srdV); gap<SQ>();
  } else {
    dma<lo, go>(d.wl, d.wg, d.voffk, d.srdK); gap<SQ>(); dma<lo + 1024, go + 128>(d.wl, d.wg, d.voffk, d.srdK); gap<SQ>();
    dma<lo + 8192, go + 8192>(d.wl, d.wg, d.voffk, d.srdK); gap<SQ>(); dma<lo + 8192 + 1024, go + 8192 + 128>(d.wl, d.wg, d.voffk, d.srdK); gap<SQ>();
  }
}
template <int A, int B> DI void load_tiles(const Ld& d) { if constexpr (A <= B) { load_tile<A>(d); load_tiles<A + 1, B>(d); } }
template <int N> DI void wait_vm() { asm volatile("s_waitcnt vmcnt(%0)" :: "i"(N) : "memory"); }
template <int B> DI void loader_steps(Ld& d) {
  if constexpr (B < NBAR) {
    constexpr int prev = B == 0 ? PRE : BAR_ISS[B - 1];
    load_tiles<prev + 1, BAR_ISS[B]>(d);
    if constexpr (B > 0) wait_vm<4 * (BAR_ISS[B] - BAR_PUB[B])>();
    bar();
    loader_steps<B + 1>(d);
  }
}

__global__ __launch_bounds__(512, 1) void gqa_attn(const unsigned short* __restrict__ Q, const unsigned short* __restrict__ K,
                                                   const unsigned short* __restrict__ V, unsigned short* __restrict__ O) {
  extern __shared__ __attribute__((aligned(16))) char smem[];
  unsigned long long pc0;
  asm volatile("s_getpc_b64 %0" : "=s"(pc0));
  const lds_ptr L = (lds_ptr)smem;
  const int tid = threadIdx.x, lane = tid & 63, r = lane & 31, h = lane >> 5;
  const int wid = __builtin_amdgcn_readfirstlane(tid >> 6), wq = wid & 3;
  const int bid = blockIdx.x, xcd = bid & 7, sl = bid >> 3, grp = xcd * 8 + (sl >> 2), hq = grp * 4 + (sl & 3);
  const unsigned rowl = (lane >> 2) & 7, sub = lane >> 5;
  const unsigned vlin = rowl * 256 + sub * 64;
  Ld d;
  u32x4 qr[8];
  unsigned pfd = 0;
  d.voffv = vlin + (lane & 3) * 16;
  d.voffk = vlin + (((lane & 3) ^ ((2 * (wq & 1) + (rowl >> 2)) & 3)) * 16);
  d.wl = (unsigned)(uintptr_t)L + wq * 2048;
  d.wg = wq * 2048;
  d.srdK = __builtin_amdgcn_make_buffer_rsrc((void*)(K + (size_t)grp * 65536), (short)0, 131072, 0x00020000);
  d.srdV = __builtin_amdgcn_make_buffer_rsrc((void*)(V + (size_t)grp * 65536), (short)0, 131072, 0x00020000);
  if (wid >= 4) {
    if (wid == 7 && lane < 32) asm volatile("global_load_dword %0, %1, %2" : "+v"(pfd) : "v"((unsigned)((sl & 7) * 2048 + lane * 64)), "s"(pc0) : "memory");
    __builtin_amdgcn_s_sleep(4);
    load_tiles<1, PRE>(d);
  } else {
    load_tile<0>(d);
    const __amdgpu_buffer_rsrc_t srdQ = __builtin_amdgcn_make_buffer_rsrc((void*)(Q + (size_t)hq * 16384), (short)0, 32768, 0x00020000);
    const unsigned v0 = vlin + (((lane & 3) ^ (rowl >> 2)) * 16), v1 = vlin + (((lane & 3) ^ (2 + (rowl >> 2))) * 16);
    const unsigned wg = wq * 8192;
    ldq<0>(qr[0], wg, v0, srdQ); ldq<2048>(qr[1], wg, v1, srdQ); ldq<4096>(qr[2], wg, v0, srdQ); ldq<6144>(qr[3], wg, v1, srdQ);
    ldq<128>(qr[4], wg, v0, srdQ); ldq<2048 + 128>(qr[5], wg, v1, srdQ); ldq<4096 + 128>(qr[6], wg, v0, srdQ); ldq<6144 + 128>(qr[7], wg, v1, srdQ);
  }
  int role = wid;
  asm volatile("" : "+s"(role));
  if (role >= 4) {
    loader_steps<0>(d);
    asm volatile("" :: "v"(pfd));
    return;
  }
  St c;
  __builtin_amdgcn_s_setprio(1);
  {
    const unsigned rb = 2048 * (r >> 3) + 64 * (r & 7);
#pragma unroll
    for (int p = 0; p < 2; ++p) {
      const unsigned a = rb + 16 * ((2 * p + h) ^ ((r >> 2) & 3));
#pragma unroll
      for (int x = 0; x < 3; ++x) { c.kb[p][x] = L + (a + x * 65536); PIN(c.kb[p][x]); }
    }
    const unsigned va = 64 * (4 * h + ((lane & 15) >> 2)) + 32 * ((lane >> 4) & 1) + 8 * (lane & 3);
#pragma unroll
    for (int x = 0; x < 3; ++x) { c.vb[x] = L + (va + x * 65536); PIN(c.vb[x]); }
    c.sw = L + (OSTG + wq * 8192 + r * 256 + 8 * h); PIN(c.sw);
    c.sr = L + (OSTG + wq * 8192 + (lane >> 3) * 256 + 16 * ((lane & 7) ^ (lane >> 3))); PIN(c.sr);
    c.rx = 16 * (r & 7); PIN(c.rx);
    c.srdO = __builtin_amdgcn_make_buffer_rsrc((void*)(O + ((size_t)hq * 128 + wq * 32) * 128), (short)0, 8192, 0x00020000);
    c.ovo = (lane >> 3) * 256 + (lane & 7) * 16; PIN(c.ovo);
    c.gq = Q + ((size_t)hq * 128 + wq * 32) * 128; c.gk = K + (size_t)grp * 65536; c.gv = V + (size_t)grp * 65536;
    lds_ptr qw = L + (QSTG + wq * 8192 + lane * 16); PIN(qw);
#define QP(i, n, off) do { wait_reg<n>(qr[i]); *(LDS_AS u32x4*)(qw + (off)) = scale_q(qr[i]); SBAR(); } while (0)
#define QRD(lo) do { CBAR(); _Pragma("unroll") for (int s = lo; s < lo + 4; ++s) \
      c.q[s] = ldsr128(c.kb[s & 1][2] + (QSTG - 131072 + 512 * (s >> 1)) + wq * 8192); SBAR(); } while (0)
    QP(0, 7, 0); QP(1, 6, 2048); QP(2, 5, 4096); QP(3, 4, 6144);
    QRD(0);
    QP(4, 3, 1024); QP(5, 2, 2048 + 1024); QP(6, 1, 4096 + 1024); QP(7, 0, 6144 + 1024);
    QRD(4);
#undef QP
#undef QRD
    bar();
    SBAR();
    first_reads(c, make_iseq<NPRE>{});
    SBAR();
  }
  slots<0>(c, make_iseq<NS>{});
  sm_ops<THRU[NS - 1]>(c, make_iseq<NOPS - THRU[NS - 1]>{});
  SBAR();
  if (__builtin_expect(__any(!(c.lsum <= BIG) || !(__builtin_fabsf(c.nm) <= LIM)), 0)) {
    asm volatile("s_waitcnt vmcnt(0) lgkmcnt(0)" ::: "memory");
    exact_path(c, L + (OSTG + wq * 8192));
  }
}

extern "C" void kernel_launch(void* const* d_in, const int* in_sizes, int n_in, void* d_out, int out_size, void* d_ws, size_t ws_size,
                              hipStream_t stream) {
  (void)hipFuncSetAttribute((const void*)gqa_attn, hipFuncAttributeMaxDynamicSharedMemorySize, LDS_BYTES);
  gqa_attn<<<256, 512, LDS_BYTES, stream>>>((const unsigned short*)d_in[0], (const unsigned short*)d_in[1], (const unsigned short*)d_in[2],
                                            (unsigned short*)d_out);
}
```
